```python
import torch

def _softmax(x, axis=-1):
    return torch.softmax(x, dim=axis)
def _gelu(x):
    return torch.nn.functional.gelu(x, approximate="tanh")
def _silu(x):
    return torch.nn.functional.silu(x)
def _sigmoid(x):
    return torch.sigmoid(x)
def _layernorm(x, axis=-1, eps=1e-5):
    m = x.mean(dim=axis, keepdim=True)
    v = x.var(dim=axis, keepdim=True, unbiased=False)
    return (x - m) / torch.sqrt(v + eps)
def _scatter_add(tgt, idx, src, axis=0):
    return tgt.clone().index_add_(axis, idx, src)
def _segment_sum(src, seg, n):
    out = torch.zeros((n,) + src.shape[1:], dtype=src.dtype)
    return out.index_add_(0, seg, src)
def _pad(x, pw):
    flat = tuple(v for pair in reversed(pw) for v in pair)
    return torch.nn.functional.pad(x, flat)

kernel_name = 'standalone__paged_block_table_kv_radixattention__decode'

def setup_inputs():

    _w = (lambda a: (lambda t: t.to(torch.bfloat16) if t.is_floating_point() else t)(torch.as_tensor(a)))
    B, H, Hkv, Dh = 4, 32, 8, 128
    Lk = 16384
    bs = 16
    nb = max(2, Lk // bs)
    P = max(1, nb // 2)
    n_phys = P + B * (nb - P)
    shared = torch.arange(P, dtype=torch.int64)
    priv = P + torch.arange(B * (nb - P), dtype=torch.int64).reshape(B, nb - P)
    bt = torch.cat([torch.tile(shared[None, :], (B, 1)), priv], dim=1)
    ctx = nb * bs - (torch.arange(1, B + 1, dtype=torch.int64) * 2654435761) % max(1, (nb * bs - P * bs) // 2)
    return {
        "q": _w((lambda *s: torch.randn(*s, dtype=torch.bfloat16) * 0.8)(B, H, Dh)),
        "k_cache": _w((lambda *s: torch.randn(*s, dtype=torch.bfloat16) * 0.8)(n_phys, bs, Hkv, Dh)),
        "v_cache": _w((lambda *s: torch.randn(*s, dtype=torch.bfloat16) * 0.05)(n_phys, bs, Hkv, Dh)),
        "block_table": _w(bt.to(torch.int64)),
        "ctx_lens": _w(torch.as_tensor(ctx).to(torch.int64)),
    }

def reference(q, k_cache, v_cache, block_table, ctx_lens):
    B, H, Dh = q.shape
    n_phys, bs, Hkv, _ = k_cache.shape
    nb = block_table.shape[1]
    Lk = nb * bs
    rep = H // Hkv
    k = k_cache[block_table].reshape(B, Lk, Hkv, Dh)
    v = v_cache[block_table].reshape(B, Lk, Hkv, Dh)
    k = (lambda x, n, axis: torch.repeat_interleave(x, n, dim=axis))(k, rep, axis=2)
    v = (lambda x, n, axis: torch.repeat_interleave(x, n, dim=axis))(v, rep, axis=2)
    scores = torch.einsum("bhd,bkhd->bhk", q, k) * (Dh ** -0.5)
    pos = torch.arange(Lk)
    valid = pos[None, :] < ctx_lens[:, None]
    scores = torch.where(valid[:, None, :], scores, -1e9)
    p = _softmax(scores, axis=-1)
    return torch.einsum("bhk,bkhd->bhd", p, v)
```

```cpp
#include <hip/hip_runtime.h>
#include <stdint.h>

namespace {
constexpr int B = 4, H = 32, HKV = 8, D = 128, BS = 16, NB = 1024, G = 4;
constexpr int LK = NB * BS;
constexpr int NSPLIT = 32;
constexpr int NP = 4;
constexpr int NK = 2;
constexpr int PLW = 2;
constexpr int NT = 4;
constexpr int NWAVE = 1 + NP + NT, NTHR = NWAVE * 64;
constexpr int RING = 16;
constexpr int MAXU = B * NB / NSPLIT;
constexpr float C2 = 0.088388347648318440f * 1.4426950408889634f;
constexpr float MINIT = -1.0e30f;
constexpr int AUX_SC1 = 16;
constexpr int RSRC3 = 0x00020000;
constexpr int W0 = 19, W1 = 22, W2 = 25, W3 = 28, W4 = 32;
constexpr int TAU = 910;
constexpr int TH = 10;
constexpr int TSC = 1024 / TH;
constexpr int T16 = 237;
constexpr int THOLD = 82, THOLDCAP = 90;
constexpr int CODE_BYTES = 8768;

typedef unsigned v4u __attribute__((ext_vector_type(4)));
typedef unsigned v2u __attribute__((ext_vector_type(2)));
typedef int i32x4 __attribute__((ext_vector_type(4)));
typedef float f32x4 __attribute__((ext_vector_type(4)));
typedef float f32x2 __attribute__((ext_vector_type(2)));
typedef short bf16x8 __attribute__((ext_vector_type(8)));
typedef short bf16x4 __attribute__((ext_vector_type(4)));
typedef __bf16 bf2 __attribute__((ext_vector_type(2)));
typedef _Float16 h2 __attribute__((ext_vector_type(2)));
typedef unsigned long long u64x8 __attribute__((ext_vector_type(8)));
#define LAS __attribute__((address_space(3)))
#define CAS __attribute__((address_space(4)))

constexpr int LDS_RING = 0;
constexpr int PREC = 1280;
constexpr int LDS_P = RING * 4096;
constexpr int LDS_Q = LDS_P + MAXU / 2 * PREC;
constexpr int LDS_BT = LDS_Q + 4096;
constexpr int LDS_ML = LDS_BT + 9 * 256;
constexpr int LDS_LIM = LDS_ML + NK * 16 * 8;
constexpr int LDS_OFF = LDS_LIM + MAXU * 4 + 16;
constexpr int LDS_CTL = LDS_OFF + MAXU * 4 + 16;
constexpr int LDS_BYTES = LDS_CTL + 64;
constexpr int LDS_CMB = LDS_RING;
constexpr int C_PROG = 0, C_NU = 2, C_DONE = 4, C_KFIN = 8, C_T0 = 10, C_TAG = 12, C_IN = 14;

constexpr int WS_EP = 0, WS_KEY = 4096, WS_DAT = WS_KEY + HKV * NSPLIT * 128, WS_END = WS_DAT + HKV * 32 * NSPLIT * 128;
constexpr unsigned SPARE = 0x00070007u;

__device__ __forceinline__ unsigned cvtpk(float lo, float hi) {
  return __builtin_bit_cast(unsigned, __builtin_convertvector(f32x2{lo, hi}, bf2));
}
__device__ __forceinline__ float max3(float a, float b, float c) { return fmaxf(fmaxf(a, b), c); }
__device__ __forceinline__ float xrow_sum(float x) {
  auto s = __builtin_amdgcn_permlane16_swap(__float_as_uint(x), __float_as_uint(x), false, false);
  x = __uint_as_float(s[0]) + __uint_as_float(s[1]);
  auto t = __builtin_amdgcn_permlane32_swap(__float_as_uint(x), __float_as_uint(x), false, false);
  return __uint_as_float(t[0]) + __uint_as_float(t[1]);
}
template <int CTRL> __device__ __forceinline__ float dppf(float x) {
  return __builtin_bit_cast(float, __builtin_amdgcn_mov_dpp(__builtin_bit_cast(int, x), CTRL, 0xf, 0xf, true));
}
__device__ __forceinline__ unsigned spread6(unsigned x) { return (x & 7u) | ((x << 13) & 0x70000u); }
struct Tag3 { unsigned a, b, c; };
__device__ __forceinline__ Tag3 tag3(unsigned tag, int piece) {
  const unsigned u = (piece & 1) ? tag >> 15 : tag;
  return Tag3{spread6((u & 0x3eu) | (tag & 1u)), spread6(u >> 6), spread6(((u >> 11) & 0x3eu) | (tag & 1u))};
}
__device__ __forceinline__ f32x4 mfma32(v4u a, v4u b, f32x4 c) {
  return __builtin_amdgcn_mfma_f32_16x16x32_bf16(__builtin_bit_cast(bf16x8, a), __builtin_bit_cast(bf16x8, b), c, 0, 0, 0);
}
__device__ __forceinline__ v2u trread(LAS char* p) { return __builtin_bit_cast(v2u, __builtin_amdgcn_ds_read_tr16_b64_v4i16((LAS bf16x4*)p)); }
__device__ __forceinline__ int lds_ldv(LAS int* p) { return __hip_atomic_load(p, __ATOMIC_RELAXED, __HIP_MEMORY_SCOPE_WORKGROUP); }
__device__ __forceinline__ int lds_ld(LAS int* p) { return __builtin_amdgcn_readfirstlane(lds_ldv(p)); }
__device__ __forceinline__ void lds_st(LAS int* p, int v) { __hip_atomic_store(p, v, __ATOMIC_RELAXED, __HIP_MEMORY_SCOPE_WORKGROUP); }
#define ORDER() asm volatile("" ::: "memory")

__global__ void __launch_bounds__(NTHR, 3)
attn_decode(const void* q_, const void* kc_, const void* vc_, const void* bt_, const void* ctx_, void* out_, void* ws_, unsigned long long kv_bytes_) {
  extern __shared__ __attribute__((aligned(16))) char smem[];
  u64x8 ka;
  asm volatile("s_load_dwordx16 %0, %1, 0x0" : "=&{s[4:19]}"(ka) : "s"(__builtin_amdgcn_kernarg_segment_ptr()));
  unsigned cpf;
  {
    const unsigned w0 = __builtin_amdgcn_readfirstlane(threadIdx.x) >> 6;
    const unsigned lo = min(((w0 + 4 * (blockIdx.x >> 3)) * 256u) & 0x3f00u, CODE_BYTES - 256u);
    const unsigned ia = LDS_CTL + (threadIdx.x & 12) * 4;
    const unsigned iv = (threadIdx.x & 12) ? 0u : ~0u;
    unsigned long long pc;
    asm volatile("s_cmp_gt_u32 %4, %c6\n\ts_cbranch_scc1 1f\n\t"
                 "ds_write_b64 %2, %3\n\tds_write_b64 %2, %3 offset:8\n\ts_branch 2f\n"
                 "1:\n\ts_getpc_b64 %1\n\ts_load_dword %0, %1, %5\n\ts_load_dword %0, %1, %5 offset:64\n\t"
                 "s_load_dword %0, %1, %5 offset:128\n\ts_load_dword %0, %1, %5 offset:192\n\ts_barrier\n"
                 "2:" : "=&{s20}"(cpf), "=&s"(pc) : "v"(ia), "v"(v2u{iv, iv}), "s"(w0), "s"(lo), "i"(NP) : "scc", "memory");
  }
  const int tid = threadIdx.x, lane = tid & 63, wave = __builtin_amdgcn_readfirstlane(tid >> 6);
  const int r = lane & 15, g = lane >> 4;
  const int h = (blockIdx.x >> 3) & 7, s = (blockIdx.x & 7) + 8 * (blockIdx.x >> 6);
  LAS int* ctl = (LAS int*)(smem + LDS_CTL);
  __builtin_amdgcn_sched_barrier(0);
  asm volatile("s_waitcnt lgkmcnt(0)\n\ts_cmp_ge_u32 %1, %c2\n\ts_cbranch_scc1 1f\n\ts_barrier\n1:"
               : "+{s[4:19]}"(ka) : "s"(wave - 1), "i"(NP), "{s20}"(cpf) : "scc", "memory");
  const char *q = (const char*)ka[0], *kc = (const char*)ka[1], *vc = (const char*)ka[2];
  const int *bt = (const int*)ka[3], *ctx = (const int*)ka[4];
  char *out = (char*)ka[5], *ws = (char*)ka[6];
  const unsigned kv_bytes = (unsigned)ka[7];
  const __amdgpu_buffer_rsrc_t rw = __builtin_amdgcn_make_buffer_rsrc((void*)ws, 0, WS_END, RSRC3);

  if (__builtin_expect(wave == 0, 1)) {
    unsigned long long now;
    asm volatile("s_memrealtime %0" : "=&{s[44:45]}"(now));
    const unsigned ctx0 = *(const CAS unsigned*)(uintptr_t)ctx;
    const CAS unsigned* bs_ = (const CAS unsigned*)(uintptr_t)(bt + s);
    const unsigned e0 = bs_[0], e1 = bs_[128], e2 = bs_[256], e3 = bs_[384], e4 = bs_[32], e5 = bs_[160];
    __builtin_amdgcn_sched_barrier(0);
    int bto = (8 * lane + (s & 7)) * 4, elo = (s + 128 * (lane & 3) + 32 * ((lane >> 2) & 3)) * 4;
    int qo[4];
#pragma unroll
    for (int t = 0; t < 4; ++t) qo[t] = ((t * H + h * G + g) * D) * 2 + (r ^ (4 * t + g)) * 16 - 1024 * t;
    asm volatile("" : "+v"(bto), "+v"(elo), "+v"(qo[0]), "+v"(qo[1]), "+v"(qo[2]), "+v"(qo[3]));
    unsigned el;
    {
      asm volatile("global_load_dword %0, %1, %2" : "=&{v28}"(el) : "v"(elo), "s"(bt));
      const v4u srdb = {(unsigned)(uintptr_t)bt, (unsigned)((uintptr_t)bt >> 32) & 0xffffu, (unsigned)(B * NB * 4), (unsigned)RSRC3};
      unsigned so;
      asm volatile("s_mov_b32 m0, %3\n\ts_movk_i32 %0, 1792\n\tbuffer_load_dword %1, %2, 0 offen lds\n\t"
                   "buffer_load_dword %1, %2, %0 offen offset:256 lds\n\ts_movk_i32 %0, 3584\n\t"
                   "buffer_load_dword %1, %2, %0 offen offset:512 lds\n\ts_movk_i32 %0, 5376\n\t"
                   "buffer_load_dword %1, %2, %0 offen offset:768 lds\n\ts_movk_i32 %0, 7168\n\t"
                   "buffer_load_dword %1, %2, %0 offen offset:1024 lds\n\ts_movk_i32 %0, 8960\n\t"
                   "buffer_load_dword %1, %2, %0 offen offset:1280 lds\n\ts_movk_i32 %0, 10752\n\t"
                   "buffer_load_dword %1, %2, %0 offen offset:1536 lds\n\ts_movk_i32 %0, 12544\n\t"
                   "buffer_load_dword %1, %2, %0 offen offset:1792 lds\n\t"
                   "global_load_lds_dword %4, %5 offset:2048 sc1"
                   : "=&s"(so) : "v"(bto), "s"(srdb), "i"(LDS_BT), "v"(h * 256), "s"(ws + WS_EP - 2048) : "memory");
      asm volatile("s_mov_b32 m0, %5\n\ts_nop 0\n\tglobal_load_lds_dwordx4 %0, %4\n\tglobal_load_lds_dwordx4 %1, %4 offset:1024\n\t"
                   "global_load_lds_dwordx4 %2, %4 offset:2048\n\tglobal_load_lds_dwordx4 %3, %4 offset:3072\n\t"
                   "s_barrier"
                   :: "v"(qo[0]), "v"(qo[1]), "v"(qo[2]), "v"(qo[3]), "s"(q), "i"(LDS_Q) : "memory");
    }
    __builtin_amdgcn_s_setprio(3);
    __builtin_amdgcn_sched_barrier(0);
    int kvo[4], vvo[4];
#pragma unroll
    for (int t = 0; t < 4; ++t) {
      vvo[t] = (4 * t + g) * 2048 + (r ^ (2 * ((4 * t + g) & 7))) * 16 - 1024 * t;
      kvo[t] = (4 * t + g) * 2048 + (r ^ (4 * t + g)) * 16 - 1024 * t;
    }
    asm volatile("" : "+v"(kvo[0]), "+v"(kvo[1]), "+v"(kvo[2]), "+v"(kvo[3]), "+v"(vvo[0]), "+v"(vvo[1]), "+v"(vvo[2]), "+v"(vvo[3]));
    const v4u srdk = {(unsigned)(uintptr_t)kc, (unsigned)((uintptr_t)kc >> 32) & 0xffffu, kv_bytes, (unsigned)RSRC3};
    const v4u srdv = {(unsigned)(uintptr_t)vc, (unsigned)((uintptr_t)vc >> 32) & 0xffffu, kv_bytes, (unsigned)RSRC3};
    unsigned pa = LDS_CTL;
    int prog = 0;
    asm volatile("" : "+v"(pa), "+v"(prog) : "s"(srdk), "s"(srdv));
    __builtin_amdgcn_sched_barrier(0);
    asm volatile("" :: "s"(e0), "s"(e1), "s"(e2), "s"(e3), "s"(e4), "s"(e5), "s"(ctx0));
#define FIRST(e_, k_) asm volatile(                        \
      "s_mov_b32 m0, %[m]\n\ts_nop 0\n\tbuffer_load_dwordx4 %[v0], %[sd], %[so] offen nt lds" \
      "\n\tbuffer_load_dwordx4 %[v1], %[sd], %[so] offen offset:1024 nt lds" \
      "\n\tbuffer_load_dwordx4 %[v2], %[sd], %[so] offen offset:2048 nt lds" \
      "\n\tbuffer_load_dwordx4 %[v3], %[sd], %[so] offen offset:3072 nt lds" \
      :: [m] "i"(LDS_RING + (k_) * 4096), [sd] "s"(srdk), [so] "s"((e_) * 32768u + h * 256), \
         [v0] "v"(kvo[0]), [v1] "v"(kvo[1]), [v2] "v"(kvo[2]), [v3] "v"(kvo[3]) : "memory")
    int gi = 0;
    const bool early = ctx0 - 1u >= (unsigned)((15 * 32 + s) * BS);
    if (early) {
      FIRST(e0, 0); FIRST(e1, 1); FIRST(e2, 2); FIRST(e3, 3); FIRST(e4, 4); FIRST(e5, 5);
      gi = 6; prog += 6;
    } else {
      asm volatile("s_waitcnt vmcnt(0)" ::: "memory");
    }
#undef FIRST
    asm volatile("s_waitcnt vmcnt(24) lgkmcnt(0)" : "+{v28}"(el), "+{s[44:45]}"(now) :: "memory");
    const unsigned t0 = (unsigned)now;
    lds_st(ctl + C_T0, (int)t0);
    lds_st(ctl + C_IN, 1);
    el = el * 32768u + h * 256;
    static_assert(LDS_RING == 0 && RING * 4096 == 0x10000 && NP == 4 && W0 >= 2 && W4 <= 36, "M0 wraps at 64 KB; four 'done' words; the ring holds the window");
    int gim = gi - RING;
    unsigned mcur = LDS_RING + gi * 4096;
    i32x4 dn = {0, 0, 0, 0};
    unsigned due = 0;
    int wlo = early ? 2 : 1, whi = wlo;
    int nu = 0;
    unsigned sv[2] = {0u, 0u};
#pragma unroll 1
    for (int ph = early ? -1 : 0; ph < 4; ++ph) {
      if (ph == 0) {
        do {
          const int nv = lds_ldv(ctl + C_NU);
          ORDER();
          sv[0] = ((const LAS unsigned*)(smem + LDS_OFF))[lane]; sv[1] = ((const LAS unsigned*)(smem + LDS_OFF))[64 + lane];
          __builtin_amdgcn_sched_barrier(0);
          nu = __builtin_amdgcn_readfirstlane(nv);
        } while (nu < 0);
        if (early) { due = (t0 + T16) * TSC - 5 * 1024; wlo = 0; whi = 4; }
      }
      int i = ph < 0 ? 6 : ph == 0 && early ? 16 : 0;
      const int n = __builtin_amdgcn_readfirstlane(ph < 0 ? 16 : (ph & 1) ? nu - 64 : min(nu, 64));
      if (i >= n) continue;
      const v4u srd = ph < 2 ? srdk : srdv;
      const unsigned lst = ph < 0 ? el : (ph & 1) ? sv[1] : sv[0];
      const int o0 = ph < 2 ? kvo[0] : vvo[0], o1 = ph < 2 ? kvo[1] : vvo[1], o2 = ph < 2 ? kvo[2] : vvo[2], o3 = ph < 2 ? kvo[3] : vvo[3];
      unsigned son = (unsigned)__builtin_amdgcn_readlane(lst, i), so, st, t2;
      int tv;
#define PUB(w, t) "\n\t.if ((" w " & 3) == " #t ")\n\tv_subrev_u32 %[tv], " w "/4, %[pr]\n\tds_write_b32 %[pa], %[tv]\n\t.endif"
#define TILE(w) \
          "s_waitcnt vmcnt(" w ")" PUB(w, 0) "\n\tbuffer_load_dwordx4 %[v0], %[sd], %[so] offen nt lds\n\t" \
          "s_add_u32 %[i], %[i], 1\n\t" \
          "v_min_i32 %[tv], v24, v25\n\t" \
          "v_readlane_b32 %[son], %[lst], %[i]\n\t" \
          "v_min3_i32 %[tv], %[tv], v26, v27\n\t" \
          "s_add_u32 %[gim], %[gim], 1\n\t" \
          "v_readfirstlane_b32 %[t2], %[tv]\n\t" \
          "s_waitcnt vmcnt(" w ") lgkmcnt(0)" PUB(w, 1) "\n\tbuffer_load_dwordx4 %[v1], %[sd], %[so] offen offset:1024 nt lds\n\t" \
          "s_mul_i32 %[st], s44, %c[sc]\n\t" \
          "ds_read_b128 %[dn], %[pa] offset:16\n\t" \
          "s_sub_u32 %[st], %[st], %[due]\n\t" \
          "s_add_u32 %[due], %[due], %c[tau]\n\t" \
          "s_ashr_i32 %[st], %[st], 11\n\t" \
          "s_max_i32 %[st], %[st], %[lo]\n\t" \
          "s_min_i32 %[st], %[st], %[hi]\n\t" \
          "s_mul_i32 %[st], %[st], .Lat1_%=-.Lat0_%=\n\t" \
          "s_waitcnt vmcnt(" w ")" PUB(w, 2) "\n\tbuffer_load_dwordx4 %[v2], %[sd], %[so] offen offset:2048 nt lds\n\t" \
          "s_cmp_ge_i32 %[gim], %[t2]\n\t" \
          "s_cselect_b32 %[st], .Latfull_%=-.Lat0_%=, %[st]\n\t" \
          "s_cmp_lt_i32 %[i], %[n]\n\t" \
          "s_cselect_b32 %[st], %[st], .Latend_%=-.Lat0_%=\n\t" \
          "s_add_u32 s46, s48, %[st]\n\t" \
          "s_addc_u32 s47, s49, 0\n\t" \
          "s_add_u32 %[mc], %[mc], 0x1000\n\t" \
          "s_waitcnt vmcnt(" w ")" PUB(w, 3) "\n\tbuffer_load_dwordx4 %[v3], %[sd], %[so] offen offset:3072 nt lds\n\t" \
          "s_memrealtime %[now]\n\t" \
          "s_and_b32 m0, %[mc], 0xffff\n\t" \
          "s_mov_b32 %[so], %[son]\n\t" \
          "v_add_u32 %[pr], 1, %[pr]\n\t" \
          "s_setpc_b64 s[46:47]\n\t.p2align 5\n"
      asm volatile(
          "s_and_b32 m0, %[mc], 0xffff\n\t"
          "s_mov_b32 %[so], %[son]\n\t"
          "s_getpc_b64 s[48:49]\n"
          ".Latpc_%=:\n\t"
          "s_add_u32 s48, s48, .Lat0_%=-.Latpc_%=\n\t"
          "s_addc_u32 s49, s49, 0\n\t"
          "s_branch .Latfull_%=\n\t.p2align 5\n"
          ".Lat0_%=:\n\t" TILE("%c[w0]")
          ".Lat1_%=:\n\t" TILE("%c[w1]")
          ".Lat2_%=:\n\t" TILE("%c[w2]")
          "\t" TILE("%c[w3]")
          "\t" TILE("%c[w4]")
          ".Latfull_%=:\n\t"
          "s_waitcnt lgkmcnt(0)\n\t"
          "v_min_i32 %[tv], v24, v25\n\t"
          "v_min3_i32 %[tv], %[tv], v26, v27\n\t"
          "ds_read_b128 %[dn], %[pa] offset:16\n\t"
          "v_readfirstlane_b32 %[t2], %[tv]\n\t"
          "s_cmp_ge_i32 %[gim], %[t2]\n\t"
          "s_cbranch_scc0 .Lat2_%=\n\t"
          "s_branch .Latfull_%=\n"
          ".Latend_%=:\n\t"
          "s_waitcnt lgkmcnt(0)"
          : [pr] "+v"(prog), [dn] "+{v[24:27]}"(dn), [now] "+{s[44:45]}"(now), [due] "+s"(due), [tv] "=&v"(tv), [st] "=&s"(st), [t2] "=&s"(t2), [so] "=&s"(so),
            [son] "+s"(son), [i] "+s"(i), [gim] "+s"(gim), [mc] "+s"(mcur)
          : [w0] "i"(W0 - 1), [w1] "i"(W1 - 1), [w2] "i"(W2 - 1), [w3] "i"(W3 - 1), [w4] "i"(W4 - 1), [tau] "i"(TAU * TSC / 64), [sc] "i"(TSC),
            [lo] "s"(wlo), [hi] "s"(whi), [pa] "v"(pa), [sd] "s"(srd), [n] "s"(n), [lst] "v"(lst), [v0] "v"(o0), [v1] "v"(o1), [v2] "v"(o2), [v3] "v"(o3)
          : "s46", "s47", "s48", "s49", "scc", "memory");
#undef TILE
#undef PUB
    }
    prog -= (W4 - 1) / 4;
#pragma unroll
    for (int k = (W4 - 1) / 4; k >= 0; --k)
      asm volatile("s_waitcnt vmcnt(%c[w])\n\tds_write_b32 %[pa], %[pr]\n\tv_add_u32 %[pr], 1, %[pr]" : [pr] "+v"(prog) : [w] "i"(4 * k), [pa] "v"(pa) : "memory");
  }
  int role = wave;
  asm volatile("" : "+s"(role));
  if (role == 0) {
  } else if (role <= NP) {
    const int pw = wave - 1;
    LAS char* const ring = (LAS char*)(smem + LDS_RING);
    LAS char* const prec = (LAS char*)(smem + LDS_P);
    int nu, pg = 0;
    if (pw < NK) {
      const int shift = (r >> 2) * 8;
      int kr[4];
#pragma unroll
      for (int t = 0; t < 4; ++t) kr[t] = r * 256 + ((4 * t + g) ^ r) * 16;
      float m = MINIT, mo = MINIT * C2, l = 0.f;
      const i32x4 craw = *(const CAS i32x4*)(uintptr_t)ctx;
      const int zq = (craw[0] <= 0 ? 1 : 0) | (craw[1] <= 0 ? 2 : 0) | (craw[2] <= 0 ? 4 : 0) | (craw[3] <= 0 ? 8 : 0);
      while (lds_ld(ctl + C_IN) == 0) __builtin_amdgcn_s_sleep(1);
      ORDER();
      v4u qf[4];
#pragma unroll
      for (int t = 0; t < 4; ++t) qf[t] = *(const LAS v4u*)(smem + LDS_Q + kr[t]);
      if ((zq >> (r >> 2)) & 1) { qf[0] = qf[1] = qf[2] = qf[3] = v4u{0u, 0u, 0u, 0u}; }
      asm volatile("" : "+v"(qf[0]), "+v"(qf[1]), "+v"(qf[2]), "+v"(qf[3]));
      do { nu = lds_ld(ctl + C_NU); } while (nu < 0);
      ORDER();
      for (int u = 2 * pw; u < nu; u += 2 * NK) {
        const int last = min(u + 1, nu - 1);
        LAS char* sa = ring + (u & (RING - 1)) * 4096;
        LAS char* sb = ring + (last & (RING - 1)) * 4096;
        const v2u lw = *(const LAS v2u*)(smem + LDS_LIM + u * 4);
        f32x4 s0, s1;
#pragma unroll
        for (int j = 0; j < 4; ++j) {
          s0[j] = (unsigned)(4 * g + j) < ((lw.x >> shift) & 0xffu) ? 0.f : -__builtin_inff();
          s1[j] = (unsigned)(4 * g + j) < ((lw.y >> shift) & 0xffu) ? 0.f : -__builtin_inff();
        }
        while (pg <= last) pg = lds_ld(ctl + C_PROG);
        ORDER();
        v4u k0[4], k1[4];
#pragma unroll
        for (int t = 0; t < 4; ++t) { k0[t] = *(const LAS v4u*)(sa + kr[t]); k1[t] = *(const LAS v4u*)(sb + kr[t]); }
#pragma unroll
        for (int t = 0; t < 4; ++t) { s0 = mfma32(k0[t], qf[t], s0); s1 = mfma32(k1[t], qf[t], s1); }
        float mn = max3(max3(s0[0], s0[1], s0[2]), max3(s0[3], s1[0], s1[1]), max3(s1[2], s1[3], m));
        asm volatile("" : "+v"(mn) :: "memory");
        lds_st(ctl + C_DONE + pw, min(u + 2 * NK, nu));
        { auto t = __builtin_amdgcn_permlane16_swap(__float_as_uint(mn), __float_as_uint(mn), false, false);
          mn = max3(__uint_as_float(t[0]), __uint_as_float(t[1]), m);
          auto w = __builtin_amdgcn_permlane32_swap(__float_as_uint(mn), __float_as_uint(mn), false, false);
          mn = max3(__uint_as_float(w[0]), __uint_as_float(w[1]), m); }
        float mc = mn * C2;
        asm volatile("" : "+v"(mc));
        const float alpha = __builtin_amdgcn_exp2f(mo - mc);
        m = mn; mo = mc;
        f32x4 p0, p1;
#pragma unroll
        for (int j = 0; j < 4; ++j) { p0[j] = __builtin_amdgcn_exp2f(fmaf(s0[j], C2, -mc)); p1[j] = __builtin_amdgcn_exp2f(fmaf(s1[j], C2, -mc)); }
        l = fmaf(l, alpha, ((p0[0] + p0[1]) + (p0[2] + p0[3])) + ((p1[0] + p1[1]) + (p1[2] + p1[3])));
        *(LAS v4u*)(prec + (u >> 1) * PREC + lane * 16) = v4u{cvtpk(p0[0], p0[1]), cvtpk(p0[2], p0[3]), cvtpk(p1[0], p1[1]), cvtpk(p1[2], p1[3])};
        *(LAS float*)(prec + (u >> 1) * PREC + 1024 + lane * 4) = mc;
      }
      ((LAS f32x2*)(smem + LDS_ML))[pw * 16 + r] = f32x2{mo, xrow_sum(l)};
      ORDER();
      lds_st(ctl + C_KFIN + pw, 1);
    } else {
      if (pw == PLW) {
        const i32x4 craw = *(const CAS i32x4*)(uintptr_t)ctx;
        const int bb = ((s >> 3) - lane) & 3, bsh = bb * 8;
        unsigned lm[2][B], mself = 0xffu << bsh, mlow = (1u << bsh) - 1u;
        unsigned own = 0x11111111u << (lane & 3), below = 0;
#pragma unroll
        for (int c = 0; c < B - 1; ++c) below |= c < bb ? 0x11111111u << (((s >> 3) - c) & 3) : 0u;
#pragma unroll
        for (int mm = 0; mm < 2; ++mm)
#pragma unroll
          for (int c = 0; c < B; ++c) {
            const int cl = (craw[c] <= 0 || craw[c] > LK) ? LK : craw[c];
            int t = cl - (mm * 512 + 8 * lane + (s & 7)) * BS; t = t < 0 ? 0 : (t > BS ? BS : t);
            lm[mm][c] = (unsigned)t << (8 * c);
          }
        const LAS unsigned* xin = (const LAS unsigned*)(smem + LDS_BT) + lane;
        const LAS unsigned* xown = xin + bb * 128;
        asm volatile("" : "+v"(lm[0][0]), "+v"(lm[0][1]), "+v"(lm[0][2]), "+v"(lm[0][3]), "+v"(lm[1][0]), "+v"(lm[1][1]), "+v"(lm[1][2]), "+v"(lm[1][3]));
        asm volatile("" : "+v"(mself), "+v"(mlow), "+v"(own), "+v"(below), "+v"(xin), "+v"(xown));
        unsigned x[2][B], xb[2], epoch;
        for (;;) {
          const int in = lds_ldv(ctl + C_IN);
          ORDER();
#pragma unroll
          for (int c = 0; c < B; ++c) { x[0][c] = xin[c * 128]; x[1][c] = xin[c * 128 + 64]; }
          xb[0] = xown[0]; xb[1] = xown[64];
          epoch = ((const LAS unsigned*)(smem + LDS_BT))[8 * 64];
          __builtin_amdgcn_sched_barrier(0);
          if (__builtin_amdgcn_readfirstlane(in)) break;
        }
        unsigned pk[2];
        bool lead[2];
        unsigned long long mask[2];
#pragma unroll
        for (int mm = 0; mm < 2; ++mm) {
          pk[mm] = (x[mm][0] == xb[mm] ? lm[mm][0] : 0u) | (x[mm][1] == xb[mm] ? lm[mm][1] : 0u) | (x[mm][2] == xb[mm] ? lm[mm][2] : 0u) | (x[mm][3] == xb[mm] ? lm[mm][3] : 0u);
          lead[mm] = ((pk[mm] & mself) != 0u) & ((pk[mm] & mlow) == 0u);
          mask[mm] = __ballot(lead[mm]);
        }
        nu = __popcll(mask[0]) + __popcll(mask[1]);
        const unsigned a0 = (unsigned)mask[0], a1 = (unsigned)(mask[0] >> 32), b0 = (unsigned)mask[1], b1 = (unsigned)(mask[1] >> 32);
        const unsigned base = __popc(a0 & below) + __popc(a1 & below) + __popc(b0 & below) + __popc(b1 & below);
        int rk[2];
        rk[0] = __builtin_amdgcn_mbcnt_hi(a1 & own, __builtin_amdgcn_mbcnt_lo(a0 & own, base));
        rk[1] = __builtin_amdgcn_mbcnt_hi(b1 & own, __builtin_amdgcn_mbcnt_lo(b0 & own, base + __popc(a0 & own) + __popc(a1 & own)));
#pragma unroll
        for (int mm = 0; mm < 2; ++mm) {
          const int g0 = rk[mm] & ~15, gn = min(nu - g0, 16), ci = rk[mm] & 3;
          int pos = g0 + __mul24(gn >> 2, ci) + min(ci, gn & 3) + ((rk[mm] >> 2) & 3);
          pos = lead[mm] ? pos : nu;
          ((LAS unsigned*)(smem + LDS_LIM))[pos] = lead[mm] ? pk[mm] : 0u;
          ((LAS unsigned*)(smem + LDS_OFF))[pos] = xb[mm] * 32768u + h * 256;
        }
        lds_st(ctl + C_TAG, (int)(epoch * 1664525u + 1013904223u));
        ORDER();
        lds_st(ctl + C_NU, nu);
      }
      while ((nu = lds_ld(ctl + C_NU)) < 0) __builtin_amdgcn_s_sleep(8);
    }
    lds_st(ctl + C_DONE + pw, nu);
    for (;;) {
      const v2u f = *(volatile LAS v2u*)(ctl + C_KFIN);
      if (__builtin_amdgcn_readfirstlane(f.x & f.y)) break;
      __builtin_amdgcn_s_sleep(2);
    }
    ORDER();
    float M, inv;
    const unsigned tag = (unsigned)lds_ld(ctl + C_TAG);
    {
      const f32x2 a = ((const LAS f32x2*)(smem + LDS_ML))[r], b = ((const LAS f32x2*)(smem + LDS_ML))[16 + r];
      M = fmaxf(a[0], b[0]);
      const float L = fmaf(__builtin_amdgcn_exp2f(a[0] - M), a[1], __builtin_amdgcn_exp2f(b[0] - M) * b[1]);
      inv = L > 0.f ? __builtin_amdgcn_rcpf(L) : 0.f;
      if (pw == NP - 1 && g == 0)
        __builtin_amdgcn_raw_buffer_store_b64(v2u{__float_as_uint(M + __builtin_amdgcn_logf(L)), tag}, rw, WS_KEY + (h * NSPLIT + s) * 128 + r * 8, 0, AUX_SC1);
    }
    int va[2];
#pragma unroll
    for (int t = 0; t < 2; ++t)
      va[t] = (4 * g + (r >> 2)) * 256 + ((2 * pw + t) ^ ((4 * g + (r >> 2)) & 7)) * 32 + (r & 3) * 8;
    f32x4 o0 = {0.f, 0.f, 0.f, 0.f}, o1 = o0;
    int doff = WS_DAT + ((h * 32 + (r & 7) * 4 + pw) * NSPLIT + s) * 128 + (r >> 3) * 64 + g * 16;
    Tag3 tg = tag3(tag, g);
    asm volatile("" : "+v"(M), "+v"(inv), "+v"(doff), "+v"(tg.a), "+v"(tg.b), "+v"(tg.c));
    for (int u = 0; u < nu; u += 2) {
      const int need = nu + min(u + 2, nu);
      LAS char* sa = ring + ((nu + u) & (RING - 1)) * 4096;
      LAS char* sb = ring + ((need - 1) & (RING - 1)) * 4096;
      v4u pb = *(const LAS v4u*)(prec + (u >> 1) * PREC + lane * 16);
      const float mref = *(const LAS float*)(prec + (u >> 1) * PREC + 1024 + lane * 4);
      LAS char *a0 = sa + va[0], *a1 = sa + va[1], *b0 = sb + va[0], *b1 = sb + va[1];
      v2u x0, x1, y0, y1;
      float beta;
      if (pg < need) {
        beta = __builtin_amdgcn_exp2f(mref - M) * inv;
        asm volatile("" : "+v"(beta), "+v"(pb), "+v"(a0), "+v"(a1), "+v"(b0), "+v"(b1));
        if (u + 6 < nu) { while ((pg = lds_ld(ctl + C_PROG)) < need) {} }
        for (;;) {
          const int pv = lds_ldv(ctl + C_PROG);
          ORDER();
          x0 = trread(a0); y0 = trread(b0); x1 = trread(a1); y1 = trread(b1);
          __builtin_amdgcn_sched_barrier(0);
          pg = __builtin_amdgcn_readfirstlane(pv);
          if (pg >= need) break;
        }
      } else {
        x0 = trread(a0); y0 = trread(b0); x1 = trread(a1); y1 = trread(b1);
        __builtin_amdgcn_sched_barrier(0);
        beta = __builtin_amdgcn_exp2f(mref - M) * inv;
      }
      const f32x4 z4 = {0.f, 0.f, 0.f, 0.f};
      const f32x4 t0 = mfma32(v4u{x0.x, x0.y, y0.x, y0.y}, pb, z4), t1 = mfma32(v4u{x1.x, x1.y, y1.x, y1.y}, pb, z4);
      __builtin_amdgcn_sched_barrier(0);
      asm volatile("" :: "v"(x0), "v"(x1), "v"(y0), "v"(y1) : "memory");
      lds_st(ctl + C_DONE + pw, need);
      o0 += beta * t0; o1 += beta * t1;
    }
    {
      const float mx = fmaxf(max3(max3(fabsf(o0[0]), fabsf(o0[1]), fabsf(o0[2])), fabsf(o0[3]), fabsf(o1[0])), max3(fabsf(o1[1]), fabsf(o1[2]), fabsf(o1[3])));
      const unsigned eb = max(__float_as_uint(mx) & 0x7e000000u, 3u << 25);
      const float sc = __uint_as_float((265u << 23) - eb);
      auto enc = [&](float lo, float hi, unsigned sp) {
        return ((__builtin_bit_cast(unsigned, __builtin_amdgcn_cvt_pkrtz(lo * sc, hi * sc)) + 0x00040004u) & ~SPARE) | sp; };
      __builtin_amdgcn_raw_buffer_store_b128(v4u{enc(o0[0], o0[1], spread6(eb >> 25)), enc(o0[2], o0[3], tg.a), enc(o1[0], o1[1], tg.b), enc(o1[2], o1[3], tg.c)},
                                             rw, doff, 0, AUX_SC1);
    }
    asm volatile("s_sleep 7\n\ts_barrier" ::: "memory");
  } else {
    const int tw = wave - 1 - NP;
    if (tw == NT - 1) {
      unsigned t = 0;
      for (unsigned o = (blockIdx.x >> 3) << 21; o < kv_bytes; o += 32u << 21) t ^= *(const CAS unsigned*)((uintptr_t)kc + o) ^ *(const CAS unsigned*)((uintptr_t)vc + o);
      asm volatile("" :: "s"(t));
    }
    int doff = WS_DAT + ((h * 32 + s) * NSPLIT + 8 * tw) * 128 + lane * 16;
    int koff = WS_KEY + (h * NSPLIT + (lane & 31)) * 128 + ((s >> 2) + 8 * (lane >> 5)) * 8;
    LAS char* wa = (LAS char*)(smem + LDS_CMB) + tw * 512 + lane * 8;
    LAS char* ra = (LAS char*)(smem + LDS_CMB) + lane * 8;
    const int row = (s >> 2) + 8 * ((lane >> 2) & 1);
    int oo = (((row >> 2) * H + h * G + (row & 3)) * D + 32 * (s & 3) + 16 * (lane >> 5) + 4 * (lane & 3) + 2 * ((lane >> 4) & 1)) * 2;
    oo = (tw == 0 && !(lane & 8)) ? oo : B * H * D * 2;
    const __amdgpu_buffer_rsrc_t ro = __builtin_amdgcn_make_buffer_rsrc((void*)out, 0, B * H * D * 2, RSRC3);
    asm volatile("" : "+v"(doff), "+v"(koff), "+v"(wa), "+v"(ra), "+v"(oo));
    int nu;
    while ((nu = lds_ld(ctl + C_NU)) < 0) __builtin_amdgcn_s_sleep(8);
    ORDER();
    const unsigned tag = (unsigned)lds_ld(ctl + C_TAG);
    Tag3 tg = tag3(tag, lane);
    while (lds_ld(ctl + C_PROG) < nu + (nu >> 1)) __builtin_amdgcn_s_sleep(32);
    unsigned spins = 0;
    v2u kg;
    for (;; ++spins) {
      ORDER();
      kg = __builtin_amdgcn_raw_buffer_load_b64(rw, koff, 0, AUX_SC1);
      if (__all(kg.y == tag) || spins > (1u << 16)) break;
      __builtin_amdgcn_s_sleep(16);
    }
    if (s == 0 && tid == NTHR - 1) __builtin_amdgcn_raw_buffer_store_b32(tag, rw, 0, WS_EP + h * 256, AUX_SC1);
    float wgt;
    {
      const float lse = __uint_as_float(kg.x);
      float M = lse;
      M = fmaxf(M, dppf<0xB1>(M)); M = fmaxf(M, dppf<0x4E>(M)); M = fmaxf(M, dppf<0x141>(M)); M = fmaxf(M, dppf<0x128>(M));
      { auto t = __builtin_amdgcn_permlane16_swap(__float_as_uint(M), __float_as_uint(M), false, false); M = fmaxf(__uint_as_float(t[0]), __uint_as_float(t[1])); }
      const float w = __builtin_amdgcn_exp2f(lse - M);
      float W = w;
      W += dppf<0xB1>(W); W += dppf<0x4E>(W); W += dppf<0x141>(W); W += dppf<0x128>(W);
      { auto t = __builtin_amdgcn_permlane16_swap(__float_as_uint(W), __float_as_uint(W), false, false); W = __uint_as_float(t[0]) + __uint_as_float(t[1]); }
      wgt = __shfl(w / W, 8 * tw + (lane >> 3) + 32 * ((lane >> 2) & 1));
    }
    {
      unsigned until = (unsigned)lds_ld(ctl + C_T0) + T16 + THOLD + ((2 * ((nu + 1) & ~1) - 16) * TAU >> 6);
      for (;;) {
        const unsigned now = (unsigned)__builtin_amdgcn_s_memrealtime();
        const int left = (int)(until - now);
        if (left <= 0) break;
        if (left > THOLDCAP && lds_ld(ctl + C_PROG) >= 2 * nu) until = now + THOLDCAP;
        if (left > 30) __builtin_amdgcn_s_sleep(6);
      }
    }
    asm volatile("s_barrier" : "+v"(wgt), "+v"(tg.a), "+v"(tg.b), "+v"(tg.c) :: "memory");
    v4u d;
    for (;; ++spins) {
      ORDER();
      d = __builtin_amdgcn_raw_buffer_load_b128(rw, doff, 0, AUX_SC1);
      if (__all((((d.y ^ tg.a) | (d.z ^ tg.b) | (d.w ^ tg.c)) & SPARE) == 0u) || spins > (1u << 17)) break;
    }
    const float f = wgt * __uint_as_float((((d.x & 7u) | ((d.x >> 13) & 0x38u)) << 25) - (11u << 23));
    float x[8];
#pragma unroll
    for (int k = 0; k < 4; ++k) { const h2 hv = __builtin_bit_cast(h2, d[k] & ~SPARE); x[2 * k] = f * (float)hv[0]; x[2 * k + 1] = f * (float)hv[1]; }
#pragma unroll
    for (int i = 0; i < 4; ++i) { auto t = __builtin_amdgcn_permlane32_swap(__float_as_uint(x[i]), __float_as_uint(x[i + 4]), false, false); x[i] = __uint_as_float(t[0]) + __uint_as_float(t[1]); }
#pragma unroll
    for (int i = 0; i < 2; ++i) { auto t = __builtin_amdgcn_permlane16_swap(__float_as_uint(x[i]), __float_as_uint(x[i + 2]), false, false); x[i] = __uint_as_float(t[0]) + __uint_as_float(t[1]); }
    *(LAS f32x2*)wa = f32x2{x[0] + dppf<0x128>(x[0]), x[1] + dppf<0x128>(x[1])};
    asm volatile("s_waitcnt lgkmcnt(0)\n\ts_barrier" ::: "memory");
    const f32x2 y = (*(const LAS f32x2*)ra + *(const LAS f32x2*)(ra + 512)) + (*(const LAS f32x2*)(ra + 1024) + *(const LAS f32x2*)(ra + 1536));
    __builtin_amdgcn_raw_buffer_store_b32(cvtpk(y[0], y[1]), ro, oo, 0, 0);
  }
}
}

extern "C" void kernel_launch(void* const* d_in, const int* in_sizes, int n_in, void* d_out, int out_size, void* d_ws, size_t ws_size,
                              hipStream_t stream) {
  (void)hipFuncSetAttribute((const void*)attn_decode, hipFuncAttributeMaxDynamicSharedMemorySize, LDS_BYTES);
  hipLaunchKernelGGL(attn_decode, dim3(HKV * NSPLIT), dim3(NTHR), LDS_BYTES, stream, (const void*)d_in[0], (const void*)d_in[1], (const void*)d_in[2],
                     (const void*)d_in[3], (const void*)d_in[4], d_out, d_ws, (unsigned long long)in_sizes[1] * 2ull);
}
```
